```python
import torch

def _softmax(x, axis=-1):
    return torch.softmax(x, dim=axis)
def _gelu(x):
    return torch.nn.functional.gelu(x, approximate="tanh")
def _silu(x):
    return torch.nn.functional.silu(x)
def _sigmoid(x):
    return torch.sigmoid(x)
def _layernorm(x, axis=-1, eps=1e-5):
    m = x.mean(dim=axis, keepdim=True)
    v = x.var(dim=axis, keepdim=True, unbiased=False)
    return (x - m) / torch.sqrt(v + eps)
def _scatter_add(tgt, idx, src, axis=0):
    return tgt.clone().index_add_(axis, idx, src)
def _segment_sum(src, seg, n):
    out = torch.zeros((n,) + src.shape[1:], dtype=src.dtype)
    return out.index_add_(0, seg, src)
def _pad(x, pw):
    flat = tuple(v for pair in reversed(pw) for v in pair)
    return torch.nn.functional.pad(x, flat)

kernel_name = 'recurrent_scan__mha_block_cached__glu_gated_swiglu__decode'

def setup_inputs():

    _w = (lambda a: (lambda t: t.to(torch.bfloat16) if t.is_floating_point() else t)(torch.as_tensor(a)))
    def _A_setup():
        B, Sq, D = 1, 1, 640
        H, Sk, Dh = 32, 32768, 192
        return {
            "x": _w((lambda *s: torch.randn(*s, dtype=torch.bfloat16) * 0.2)(B, Sq, D)),
            "k_cache": _w((lambda *s: torch.randn(*s, dtype=torch.bfloat16) * 1.6)(B, H, Sk, Dh)),
            "v_cache": _w((lambda *s: torch.randn(*s, dtype=torch.bfloat16) * 1.6)(B, H, Sk, Dh)),
            "wq": _w((lambda *s: torch.randn(*s, dtype=torch.bfloat16) * 1.6)(D, H * Dh) * (D ** -0.5)),
            "wo": _w((lambda *s: torch.randn(*s, dtype=torch.bfloat16) * 1.6)(H * Dh, D) * ((H * Dh) ** -0.5)),
        }
    _A_in = _A_setup()

    def _B_setup():
        B, S, D, F = 1, 1, 640, 1536
        return {
            "x": _w((lambda *s: torch.randn(*s, dtype=torch.bfloat16) * 0.2)(B, S, D)),
            "w1": _w((lambda *s: torch.randn(*s, dtype=torch.bfloat16) * 0.8)(D, F) * (2.0 / D) ** 0.5),
            "w3": _w((lambda *s: torch.randn(*s, dtype=torch.bfloat16) * 0.8)(D, F) * (2.0 / D) ** 0.5),
            "w2": _w((lambda *s: torch.randn(*s, dtype=torch.bfloat16) * 0.8)(F, D) * (2.0 / F) ** 0.5),
        }
    _B_in = _B_setup()

    return {
        "A_x": _A_in["x"],
        "A_k_cache": _A_in["k_cache"],
        "A_v_cache": _A_in["v_cache"],
        "A_wq": _A_in["wq"],
        "A_wo": _A_in["wo"],
        "B_x": _B_in["x"],
        "B_w1": _B_in["w1"],
        "B_w3": _B_in["w3"],
        "B_w2": _B_in["w2"],
    }

def reference(A_x, A_k_cache, A_v_cache, A_wq, A_wo, B_x, B_w1, B_w3, B_w2):
    def _A_ref(x, k_cache, v_cache, wq, wo):
        B, Sq, D = x.shape
        H, Dh = 32, 192
        q = (lambda x, *d: x.permute(*d))((x @ wq).reshape(B, Sq, H, Dh), 0, 2, 1, 3)
        scores = torch.einsum("bhqd,bhkd->bhqk", q, k_cache) * (Dh ** -0.5)
        p = _softmax(scores, axis=-1)
        o = torch.einsum("bhqk,bhkd->bhqd", p, v_cache)
        return (lambda x, *d: x.permute(*d))(o, 0, 2, 1, 3).reshape(B, Sq, H * Dh) @ wo

    def _B_ref(x, w1, w3, w2):
        gate = torch.einsum("bsd,df->bsf", x, w1)
        up = torch.einsum("bsd,df->bsf", x, w3)
        h = _silu(gate) * up
        out = torch.einsum("bsf,fd->bsd", h, w2)
        return out

    x = A_x
    xn = _layernorm(x, axis=-1)
    s = _A_ref(xn, A_k_cache, A_v_cache, A_wq, A_wo)
    g = _silu(_B_ref(xn, B_w1, B_w3, B_w2))
    return x + s * g
```

```cpp
#include <hip/hip_runtime.h>
#include <hip/hip_bf16.h>
#include <cstdint>

namespace {

constexpr int D_MODEL = 640, NH = 32, SK = 32768, DH = 192, FF = 1536, HD = NH * DH;
#define NSPLIT 16
constexpr int NSP = NSPLIT;
constexpr int KEYS_PER_WG = SK / NSP;
constexpr int PREC = 200;
constexpr size_t WS_Q = 0;
constexpr size_t WS_H = 16384;
constexpr size_t WS_CNT = 32768;
constexpr size_t WS_PART = 65536;
constexpr size_t WS_CP = WS_PART + (size_t)NH * 64 * PREC * 4;

using u32x4 = __attribute__((ext_vector_type(4))) unsigned;
using f32x4 = __attribute__((ext_vector_type(4))) float;
using bf16x2 = __attribute__((ext_vector_type(2))) __bf16;

__device__ __forceinline__ float bf2f(unsigned short b) { return __uint_as_float(((unsigned)b) << 16); }
__device__ __forceinline__ float rbf(float x) {
  __hip_bfloat16 b = __float2bfloat16(x);
  return __bfloat162float(b);
}
__device__ __forceinline__ unsigned short f2bfbits(float x) {
  __hip_bfloat16 b = __float2bfloat16(x);
  return __builtin_bit_cast(unsigned short, b);
}
__device__ __forceinline__ float dot2(unsigned k, unsigned q, float acc) {
  return __builtin_amdgcn_fdot2_f32_bf16(__builtin_bit_cast(bf16x2, k), __builtin_bit_cast(bf16x2, q), acc, false);
}
template <int CTRL> __device__ __forceinline__ float dpp(float x) {
  return __builtin_bit_cast(float, __builtin_amdgcn_mov_dpp(__builtin_bit_cast(int, x), CTRL, 0xf, 0xf, true));
}
__device__ __forceinline__ float lo_f(unsigned v) { return __uint_as_float(v << 16); }
__device__ __forceinline__ float hi_f(unsigned v) { return __uint_as_float(v & 0xffff0000u); }
__device__ __forceinline__ float silu_f(float x) { return x / (1.f + __expf(-x)); }

__device__ __forceinline__ void layernorm_to_lds(const __hip_bfloat16* __restrict__ x, float* xn, float* red) {
  const int t = threadIdx.x, lane = t & 63, w = t >> 6;
  float v0 = __bfloat162float(x[t]), v1 = __bfloat162float(x[t + 256]);
  float v2 = (t < 128) ? __bfloat162float(x[t + 512]) : 0.f;
  float s = v0 + v1 + v2;
  for (int o = 32; o; o >>= 1) s += __shfl_xor(s, o);
  if (lane == 0) red[w] = s;
  __syncthreads();
  const float mean = (red[0] + red[1] + red[2] + red[3]) * (1.f / D_MODEL);
  float d0 = v0 - mean, d1 = v1 - mean, d2 = (t < 128) ? v2 - mean : 0.f;
  float q = d0 * d0 + d1 * d1 + d2 * d2;
  for (int o = 32; o; o >>= 1) q += __shfl_xor(q, o);
  if (lane == 0) red[4 + w] = q;
  __syncthreads();
  const float var = (red[4] + red[5] + red[6] + red[7]) * (1.f / D_MODEL);
  const float mb = rbf(mean), vb = rbf(var);
  const float den = rbf(sqrtf(rbf(vb + 1e-5f)));
  xn[t] = rbf(rbf(v0 - mb) / den);
  xn[t + 256] = rbf(rbf(v1 - mb) / den);
  if (t < 128) xn[t + 512] = rbf(rbf(v2 - mb) / den);
  __syncthreads();
}

__global__ void __launch_bounds__(256) kA(const __hip_bfloat16* __restrict__ x, const __hip_bfloat16* __restrict__ wq,
                                          const __hip_bfloat16* __restrict__ w1, const __hip_bfloat16* __restrict__ w3,
                                          unsigned char* __restrict__ ws) {
  __shared__ float xn[D_MODEL];
  __shared__ float red[8];
  __shared__ float part[2][4][32];
  const int t = threadIdx.x, lane = t & 63, w = t >> 6;
  const int rg = t >> 2, c = t & 3;
  const bool isq = blockIdx.x < 192;
  if (blockIdx.x == 0 && t < 5) reinterpret_cast<unsigned*>(ws + WS_CNT)[t * 64] = 0u;
  u32x4 a[10], b[10];
  if (isq) {
    const __hip_bfloat16* p = wq + (size_t)rg * HD + blockIdx.x * 32 + c * 8;
#pragma unroll
    for (int i = 0; i < 10; ++i) a[i] = *reinterpret_cast<const u32x4*>(p + (size_t)i * 64 * HD);
  } else {
    const int cb = (blockIdx.x - 192) * 32 + c * 8;
    const __hip_bfloat16* p1 = w1 + (size_t)rg * FF + cb;
    const __hip_bfloat16* p3 = w3 + (size_t)rg * FF + cb;
#pragma unroll
    for (int i = 0; i < 10; ++i) { a[i] = *reinterpret_cast<const u32x4*>(p1 + (size_t)i * 64 * FF); b[i] = *reinterpret_cast<const u32x4*>(p3 + (size_t)i * 64 * FF); }
  }
  layernorm_to_lds(x, xn, red);
  float acc[8] = {0, 0, 0, 0, 0, 0, 0, 0}, acc2[8] = {0, 0, 0, 0, 0, 0, 0, 0};
#pragma unroll
  for (int i = 0; i < 10; ++i) {
    const float xv = xn[rg + 64 * i];
#pragma unroll
    for (int k = 0; k < 4; ++k) { acc[2 * k] = fmaf(xv, lo_f(a[i][k]), acc[2 * k]); acc[2 * k + 1] = fmaf(xv, hi_f(a[i][k]), acc[2 * k + 1]); }
  }
  if (!isq) {
#pragma unroll
    for (int i = 0; i < 10; ++i) {
      const float xv = xn[rg + 64 * i];
#pragma unroll
      for (int k = 0; k < 4; ++k) { acc2[2 * k] = fmaf(xv, lo_f(b[i][k]), acc2[2 * k]); acc2[2 * k + 1] = fmaf(xv, hi_f(b[i][k]), acc2[2 * k + 1]); }
    }
  }
#pragma unroll
  for (int k = 0; k < 8; ++k) {
    for (int o = 4; o < 64; o <<= 1) { acc[k] += __shfl_xor(acc[k], o); acc2[k] += __shfl_xor(acc2[k], o); }
  }
  if (lane < 4) {
#pragma unroll
    for (int k = 0; k < 8; ++k) { part[0][w][c * 8 + k] = acc[k]; part[1][w][c * 8 + k] = acc2[k]; }
  }
  __syncthreads();
  if (t < 32) {
    const float s0 = part[0][0][t] + part[0][1][t] + part[0][2][t] + part[0][3][t];
    if (isq) {
      reinterpret_cast<unsigned short*>(ws + WS_Q)[blockIdx.x * 32 + t] = f2bfbits(s0);
    } else {
      const float s1 = part[1][0][t] + part[1][1][t] + part[1][2][t] + part[1][3][t];
      const float g = rbf(s0), u = rbf(s1);
      reinterpret_cast<float*>(ws + WS_H)[(blockIdx.x - 192) * 32 + t] = rbf(rbf(silu_f(g)) * u);
    }
  }
}

constexpr int UB = 2;
constexpr int ROWS_IT = 8 * UB;
constexpr int WAVES_B = 4;
constexpr int KEYS_PER_WAVE = KEYS_PER_WG / WAVES_B;
constexpr int NIT = KEYS_PER_WAVE / ROWS_IT;

__global__ void __launch_bounds__(256, 2) kB(const __hip_bfloat16* __restrict__ kc, const __hip_bfloat16* __restrict__ vc,
                                             unsigned char* __restrict__ ws) {
  __shared__ __attribute__((aligned(16))) float mrg[WAVES_B][PREC];
  const int h = blockIdx.x / NSP, sp = blockIdx.x % NSP;
  const int t = threadIdx.x, lane = t & 63, wave = __builtin_amdgcn_readfirstlane(t >> 6);
  const int r = lane >> 3, c = lane & 7;
  constexpr float SCALE = 0.07216878364870322f;
  constexpr float LOG2E = 1.4426950408889634f;
  u32x4 qf[3];
  {
    const unsigned short* q = reinterpret_cast<const unsigned short*>(ws + WS_Q) + h * DH + c * 8;
#pragma unroll
    for (int j = 0; j < 3; ++j) qf[j] = *reinterpret_cast<const u32x4*>(q + 64 * j);
  }
  const size_t key0 = (size_t)h * SK + (size_t)sp * KEYS_PER_WG + (size_t)wave * KEYS_PER_WAVE;
  const __hip_bfloat16* kp = kc + (key0 + r) * DH + c * 8;
  const __hip_bfloat16* vp = vc + (key0 + r) * DH + c * 8;
  u32x4 Kn[UB][3], Vn[UB][3];
#define ISSUE(it_) do { const __hip_bfloat16* kq_ = kp + (size_t)(it_) * ROWS_IT * DH; const __hip_bfloat16* vq_ = vp + (size_t)(it_) * ROWS_IT * DH; \
    _Pragma("unroll") for (int u = 0; u < UB; ++u) _Pragma("unroll") for (int j = 0; j < 3; ++j) Kn[u][j] = __builtin_nontemporal_load(reinterpret_cast<const u32x4*>(kq_ + u * 8 * DH + 64 * j)); \
    _Pragma("unroll") for (int u = 0; u < UB; ++u) _Pragma("unroll") for (int j = 0; j < 3; ++j) Vn[u][j] = __builtin_nontemporal_load(reinterpret_cast<const u32x4*>(vq_ + u * 8 * DH + 64 * j)); } while (0)
  float m = -INFINITY, l = 0.f, acc[24];
#pragma unroll
  for (int k = 0; k < 24; ++k) acc[k] = 0.f;
  ISSUE(0);
  for (int it = 0; it < NIT; ++it) {
    u32x4 Kc[UB][3], Vc[UB][3];
#pragma unroll
    for (int u = 0; u < UB; ++u)
#pragma unroll
      for (int j = 0; j < 3; ++j) { Kc[u][j] = Kn[u][j]; Vc[u][j] = Vn[u][j]; }
    if (it + 1 < NIT) ISSUE(it + 1);
    float s[UB];
#pragma unroll
    for (int u = 0; u < UB; ++u) {
      float d = 0.f;
#pragma unroll
      for (int j = 0; j < 3; ++j)
#pragma unroll
        for (int k = 0; k < 4; ++k) d = dot2(Kc[u][j][k], qf[j][k], d);
      d += dpp<0xB1>(d);
      d += dpp<0x4E>(d);
      d += dpp<0x141>(d);
      s[u] = rbf(rbf(d) * SCALE) * LOG2E;
    }
    float mn = m;
#pragma unroll
    for (int u = 0; u < UB; ++u) mn = fmaxf(mn, s[u]);
    const float alpha = __builtin_amdgcn_exp2f(m - mn);
    m = mn;
    float p[UB];
    float ls = 0.f;
#pragma unroll
    for (int u = 0; u < UB; ++u) { p[u] = __builtin_amdgcn_exp2f(s[u] - mn); ls += p[u]; }
    l = fmaf(l, alpha, ls);
#pragma unroll
    for (int k = 0; k < 24; ++k) acc[k] *= alpha;
#pragma unroll
    for (int u = 0; u < UB; ++u)
#pragma unroll
      for (int j = 0; j < 3; ++j)
#pragma unroll
        for (int k = 0; k < 4; ++k) {
          acc[j * 8 + 2 * k] = fmaf(p[u], lo_f(Vc[u][j][k]), acc[j * 8 + 2 * k]);
          acc[j * 8 + 2 * k + 1] = fmaf(p[u], hi_f(Vc[u][j][k]), acc[j * 8 + 2 * k + 1]);
        }
  }
#undef ISSUE
  float M = m;
  for (int o = 8; o < 64; o <<= 1) M = fmaxf(M, __shfl_xor(M, o));
  const float sc = __builtin_amdgcn_exp2f(m - M);
  l *= sc;
  for (int o = 8; o < 64; o <<= 1) l += __shfl_xor(l, o);
#pragma unroll
  for (int k = 0; k < 24; ++k) {
    float a = acc[k] * sc;
    for (int o = 8; o < 64; o <<= 1) a += __shfl_xor(a, o);
    acc[k] = a;
  }
  if (r == 0) {
#pragma unroll
    for (int j = 0; j < 3; ++j) {
      *reinterpret_cast<f32x4*>(&mrg[wave][8 + 64 * j + 8 * c]) = f32x4{acc[j * 8], acc[j * 8 + 1], acc[j * 8 + 2], acc[j * 8 + 3]};
      *reinterpret_cast<f32x4*>(&mrg[wave][8 + 64 * j + 8 * c + 4]) = f32x4{acc[j * 8 + 4], acc[j * 8 + 5], acc[j * 8 + 6], acc[j * 8 + 7]};
    }
    if (c == 0) { mrg[wave][0] = M; mrg[wave][1] = l; }
  }
  __syncthreads();
  if (t < DH) {
    float MM = -INFINITY;
#pragma unroll
    for (int w = 0; w < WAVES_B; ++w) MM = fmaxf(MM, mrg[w][0]);
    float L = 0.f, o = 0.f;
#pragma unroll
    for (int w = 0; w < WAVES_B; ++w) {
      const float wt = __builtin_amdgcn_exp2f(mrg[w][0] - MM);
      L = fmaf(wt, mrg[w][1], L);
      o = fmaf(wt, mrg[w][8 + t], o);
    }
    float* rec = reinterpret_cast<float*>(ws + WS_PART) + ((size_t)h * NSP + sp) * PREC;
    rec[8 + t] = o;
    if (t == 0) { rec[0] = MM; rec[1] = L; }
  }
}

__global__ void __launch_bounds__(256) kC(const __hip_bfloat16* __restrict__ x, const __hip_bfloat16* __restrict__ wo,
                                          const __hip_bfloat16* __restrict__ w2, unsigned char* __restrict__ ws,
                                          __hip_bfloat16* __restrict__ out) {
  __shared__ __attribute__((aligned(16))) float vec[DH];
  __shared__ __attribute__((aligned(16))) float part[4][128];
  __shared__ unsigned lastflag;
  const int ct = blockIdx.x % 5, rt = blockIdx.x / 5;
  const int t = threadIdx.x, lane = t & 63, w = t >> 6;
  const int rs = t >> 4, c = t & 15;
  const __hip_bfloat16* W = (rt < NH) ? wo + (size_t)rt * DH * D_MODEL : w2 + (size_t)(rt - NH) * DH * D_MODEL;
  u32x4 a[12];
  {
    const __hip_bfloat16* p = W + (size_t)rs * D_MODEL + ct * 128 + c * 8;
#pragma unroll
    for (int i = 0; i < 12; ++i) a[i] = *reinterpret_cast<const u32x4*>(p + (size_t)i * 16 * D_MODEL);
  }
  if (t < DH) {
    if (rt < NH) {
      const float* rec = reinterpret_cast<const float*>(ws + WS_PART) + (size_t)rt * NSP * PREC;
      float MM = -INFINITY;
      for (int s = 0; s < NSP; ++s) MM = fmaxf(MM, rec[s * PREC]);
      float L = 0.f, o = 0.f;
      for (int s = 0; s < NSP; ++s) {
        const float wt = __builtin_amdgcn_exp2f(rec[s * PREC] - MM);
        L = fmaf(wt, rec[s * PREC + 1], L);
        o = fmaf(wt, rec[s * PREC + 8 + t], o);
      }
      vec[t] = rbf(o / L);
    } else {
      vec[t] = reinterpret_cast<const float*>(ws + WS_H)[(rt - NH) * DH + t];
    }
  }
  __syncthreads();
  float acc[8] = {0, 0, 0, 0, 0, 0, 0, 0};
#pragma unroll
  for (int i = 0; i < 12; ++i) {
    const float xv = vec[rs + 16 * i];
#pragma unroll
    for (int k = 0; k < 4; ++k) { acc[2 * k] = fmaf(xv, lo_f(a[i][k]), acc[2 * k]); acc[2 * k + 1] = fmaf(xv, hi_f(a[i][k]), acc[2 * k + 1]); }
  }
#pragma unroll
  for (int k = 0; k < 8; ++k) { acc[k] += __shfl_xor(acc[k], 16); acc[k] += __shfl_xor(acc[k], 32); }
  if (lane < 16) {
#pragma unroll
    for (int k = 0; k < 8; ++k) part[w][c * 8 + k] = acc[k];
  }
  __syncthreads();
  float* cp = reinterpret_cast<float*>(ws + WS_CP) + (size_t)ct * 40 * 128;
  if (t < 128) {
    const float s = part[0][t] + part[1][t] + part[2][t] + part[3][t];
    __hip_atomic_store(reinterpret_cast<unsigned*>(cp + rt * 128 + t), __float_as_uint(s), __ATOMIC_RELAXED, __HIP_MEMORY_SCOPE_AGENT);
  }
  asm volatile("s_waitcnt vmcnt(0)" ::: "memory");
  __syncthreads();
  if (t == 0) {
    unsigned* cnt = reinterpret_cast<unsigned*>(ws + WS_CNT) + ct * 64;
    const unsigned old = __hip_atomic_fetch_add(cnt, 1u, __ATOMIC_RELAXED, __HIP_MEMORY_SCOPE_AGENT);
    const unsigned last = (old == 39u) ? 1u : 0u;
    if (last) { __builtin_amdgcn_fence(__ATOMIC_ACQUIRE, "agent"); asm volatile("s_waitcnt vmcnt(0)" ::: "memory"); }
    lastflag = last;
  }
  __syncthreads();
  if (lastflag == 0u) return;
  if (t < 128) {
    float s = 0.f, f = 0.f;
    for (int i = 0; i < NH; ++i) s += __uint_as_float(__hip_atomic_load(reinterpret_cast<unsigned*>(cp + i * 128 + t), __ATOMIC_RELAXED, __HIP_MEMORY_SCOPE_AGENT));
    for (int i = NH; i < 40; ++i) f += __uint_as_float(__hip_atomic_load(reinterpret_cast<unsigned*>(cp + i * 128 + t), __ATOMIC_RELAXED, __HIP_MEMORY_SCOPE_AGENT));
    const float g = rbf(silu_f(rbf(f)));
    const int col = ct * 128 + t;
    out[col] = __float2bfloat16(__bfloat162float(x[col]) + rbf(rbf(s) * g));
  }
}

}

extern "C" void kernel_launch(void* const* d_in, const int* in_sizes, int n_in, void* d_out, int out_size, void* d_ws,
                              size_t ws_size, hipStream_t stream) {
  const __hip_bfloat16* x = (const __hip_bfloat16*)d_in[0];
  const __hip_bfloat16* kc = (const __hip_bfloat16*)d_in[1];
  const __hip_bfloat16* vc = (const __hip_bfloat16*)d_in[2];
  const __hip_bfloat16* wq = (const __hip_bfloat16*)d_in[3];
  const __hip_bfloat16* wo = (const __hip_bfloat16*)d_in[4];
  const __hip_bfloat16* w1 = (const __hip_bfloat16*)d_in[6];
  const __hip_bfloat16* w3 = (const __hip_bfloat16*)d_in[7];
  const __hip_bfloat16* w2 = (const __hip_bfloat16*)d_in[8];
  unsigned char* ws = (unsigned char*)d_ws;
  hipLaunchKernelGGL(kA, dim3(240), dim3(256), 0, stream, x, wq, w1, w3, ws);
  hipLaunchKernelGGL(kB, dim3(NH * NSP), dim3(256), 0, stream, kc, vc, ws);
  hipLaunchKernelGGL(kC, dim3(200), dim3(256), 0, stream, x, wo, w2, ws, (__hip_bfloat16*)d_out);
}
```

```cpp
#include <hip/hip_runtime.h>
#include <hip/hip_bf16.h>
#include <cstdint>

namespace {

constexpr int D_MODEL = 640, NH = 32, SK = 32768, DH = 192, FF = 1536, HD = NH * DH;
#define NSPLIT 16
constexpr int NSP = NSPLIT;
constexpr int KEYS_PER_WG = SK / NSP;
constexpr int PREC = 200;
constexpr size_t WS_Q = 0;
constexpr size_t WS_H = 16384;
constexpr size_t WS_CNT = 32768;
constexpr size_t WS_PART = 65536;
constexpr size_t WS_CP = WS_PART + (size_t)NH * 64 * PREC * 4;
constexpr size_t WS_QP = WS_CP + 5 * 40 * 128 * 4;
constexpr size_t WS_GP = WS_QP + (size_t)8 * HD * 4;

using u32x4 = __attribute__((ext_vector_type(4))) unsigned;
using f32x4 = __attribute__((ext_vector_type(4))) float;
using bf16x2 = __attribute__((ext_vector_type(2))) __bf16;

__device__ __forceinline__ float bf2f(unsigned short b) { return __uint_as_float(((unsigned)b) << 16); }
__device__ __forceinline__ float rbf(float x) {
  __hip_bfloat16 b = __float2bfloat16(x);
  return __bfloat162float(b);
}
__device__ __forceinline__ unsigned short f2bfbits(float x) {
  __hip_bfloat16 b = __float2bfloat16(x);
  return __builtin_bit_cast(unsigned short, b);
}
__device__ __forceinline__ float dot2(unsigned k, unsigned q, float acc) {
  return __builtin_amdgcn_fdot2_f32_bf16(__builtin_bit_cast(bf16x2, k), __builtin_bit_cast(bf16x2, q), acc, false);
}
template <int CTRL> __device__ __forceinline__ float dpp(float x) {
  return __builtin_bit_cast(float, __builtin_amdgcn_mov_dpp(__builtin_bit_cast(int, x), CTRL, 0xf, 0xf, true));
}
__device__ __forceinline__ float lo_f(unsigned v) { return __uint_as_float(v << 16); }
__device__ __forceinline__ float hi_f(unsigned v) { return __uint_as_float(v & 0xffff0000u); }
__device__ __forceinline__ float silu_f(float x) { return x / (1.f + __expf(-x)); }

__device__ __forceinline__ void layernorm_to_lds(const __hip_bfloat16* __restrict__ x, float* xn, float* red) {
  const int t = threadIdx.x, lane = t & 63, w = t >> 6;
  float v0 = __bfloat162float(x[t]), v1 = __bfloat162float(x[t + 256]);
  float v2 = (t < 128) ? __bfloat162float(x[t + 512]) : 0.f;
  float s = v0 + v1 + v2;
  for (int o = 32; o; o >>= 1) s += __shfl_xor(s, o);
  if (lane == 0) red[w] = s;
  __syncthreads();
  const float mean = (red[0] + red[1] + red[2] + red[3]) * (1.f / D_MODEL);
  float d0 = v0 - mean, d1 = v1 - mean, d2 = (t < 128) ? v2 - mean : 0.f;
  float q = d0 * d0 + d1 * d1 + d2 * d2;
  for (int o = 32; o; o >>= 1) q += __shfl_xor(q, o);
  if (lane == 0) red[4 + w] = q;
  __syncthreads();
  const float var = (red[4] + red[5] + red[6] + red[7]) * (1.f / D_MODEL);
  const float mb = rbf(mean), vb = rbf(var);
  const float den = rbf(sqrtf(rbf(vb + 1e-5f)));
  xn[t] = rbf(rbf(v0 - mb) / den);
  xn[t + 256] = rbf(rbf(v1 - mb) / den);
  if (t < 128) xn[t + 512] = rbf(rbf(v2 - mb) / den);
  __syncthreads();
}

__global__ void __launch_bounds__(256) kA(const __hip_bfloat16* __restrict__ x, const __hip_bfloat16* __restrict__ wq,
                                          const __hip_bfloat16* __restrict__ w1, const __hip_bfloat16* __restrict__ w3,
                                          unsigned char* __restrict__ ws) {
  __shared__ float xn[D_MODEL];
  __shared__ float red[8];
  __shared__ __attribute__((aligned(16))) float part[4][DH];
  const int t = threadIdx.x, lane = t & 63, w = t >> 6;
  const int rg = t >> 3, c = t & 7;
  if (blockIdx.x == 0 && t < 5) reinterpret_cast<unsigned*>(ws + WS_CNT)[t * 64] = 0u;
  const int b = blockIdx.x, sl = b & 7;
  const __hip_bfloat16* W; int ld, col0; float* dst;
  if (b < 256) { W = wq; ld = HD; col0 = (b >> 3) * DH; dst = reinterpret_cast<float*>(ws + WS_QP) + sl * HD + col0; }
  else { const int m = (b - 256) >> 6, tile = ((b - 256) >> 3) & 7; W = m ? w3 : w1; ld = FF; col0 = tile * DH;
         dst = reinterpret_cast<float*>(ws + WS_GP) + (m * 8 + sl) * FF + col0; }
  const __hip_bfloat16* p = W + (size_t)(sl * 80 + rg) * ld + col0 + c * 8;
  u32x4 a[3][3];
#pragma unroll
  for (int i = 0; i < 3; ++i)
#pragma unroll
    for (int j = 0; j < 3; ++j)
      a[i][j] = (i < 2 || rg < 16) ? *reinterpret_cast<const u32x4*>(p + (size_t)i * 32 * ld + 64 * j) : u32x4{0u, 0u, 0u, 0u};
  layernorm_to_lds(x, xn, red);
  float acc[24];
#pragma unroll
  for (int k = 0; k < 24; ++k) acc[k] = 0.f;
#pragma unroll
  for (int i = 0; i < 3; ++i) {
    const float xv = (i < 2 || rg < 16) ? xn[sl * 80 + rg + 32 * i] : 0.f;
#pragma unroll
    for (int j = 0; j < 3; ++j)
#pragma unroll
      for (int k = 0; k < 4; ++k) { acc[j * 8 + 2 * k] = fmaf(xv, lo_f(a[i][j][k]), acc[j * 8 + 2 * k]); acc[j * 8 + 2 * k + 1] = fmaf(xv, hi_f(a[i][j][k]), acc[j * 8 + 2 * k + 1]); }
  }
#pragma unroll
  for (int k = 0; k < 24; ++k) for (int o = 8; o < 64; o <<= 1) acc[k] += __shfl_xor(acc[k], o);
  if (lane < 8) {
#pragma unroll
    for (int j = 0; j < 3; ++j)
#pragma unroll
      for (int k = 0; k < 8; ++k) part[w][64 * j + 8 * c + k] = acc[j * 8 + k];
  }
  __syncthreads();
  if (t < DH) dst[t] = part[0][t] + part[1][t] + part[2][t] + part[3][t];
}

constexpr int UB = 2;
constexpr int ROWS_IT = 8 * UB;
constexpr int WAVES_B = 4;
constexpr int KEYS_PER_WAVE = KEYS_PER_WG / WAVES_B;
constexpr int NIT = KEYS_PER_WAVE / ROWS_IT;

__global__ void __launch_bounds__(256, 2) kB(const __hip_bfloat16* __restrict__ kc, const __hip_bfloat16* __restrict__ vc,
                                             unsigned char* __restrict__ ws) {
  __shared__ __attribute__((aligned(16))) float mrg[WAVES_B][PREC];
  const int h = blockIdx.x / NSP, sp = blockIdx.x % NSP;
  const int t = threadIdx.x, lane = t & 63, wave = __builtin_amdgcn_readfirstlane(t >> 6);
  const int r = lane >> 3, c = lane & 7;
  constexpr float SCALE = 0.07216878364870322f;
  constexpr float LOG2E = 1.4426950408889634f;
  u32x4 qf[3];
  {
    const float* qp = reinterpret_cast<const float*>(ws + WS_QP) + h * DH + c * 8;
#pragma unroll
    for (int j = 0; j < 3; ++j) {
      f32x4 s0 = {0.f, 0.f, 0.f, 0.f}, s1 = {0.f, 0.f, 0.f, 0.f};
#pragma unroll
      for (int sl = 0; sl < 8; ++sl) { s0 += *reinterpret_cast<const f32x4*>(qp + sl * HD + 64 * j); s1 += *reinterpret_cast<const f32x4*>(qp + sl * HD + 64 * j + 4); }
      qf[j] = u32x4{(unsigned)f2bfbits(s0[0]) | ((unsigned)f2bfbits(s0[1]) << 16), (unsigned)f2bfbits(s0[2]) | ((unsigned)f2bfbits(s0[3]) << 16),
                    (unsigned)f2bfbits(s1[0]) | ((unsigned)f2bfbits(s1[1]) << 16), (unsigned)f2bfbits(s1[2]) | ((unsigned)f2bfbits(s1[3]) << 16)};
    }
  }
  const size_t key0 = (size_t)h * SK + (size_t)sp * KEYS_PER_WG + (size_t)wave * KEYS_PER_WAVE;
  const __hip_bfloat16* kp = kc + (key0 + r) * DH + c * 8;
  const __hip_bfloat16* vp = vc + (key0 + r) * DH + c * 8;
  u32x4 Kn[UB][3], Vn[UB][3];
#define ISSUE(it_) do { const __hip_bfloat16* kq_ = kp + (size_t)(it_) * ROWS_IT * DH; const __hip_bfloat16* vq_ = vp + (size_t)(it_) * ROWS_IT * DH; \
    _Pragma("unroll") for (int u = 0; u < UB; ++u) _Pragma("unroll") for (int j = 0; j < 3; ++j) Kn[u][j] = __builtin_nontemporal_load(reinterpret_cast<const u32x4*>(kq_ + u * 8 * DH + 64 * j)); \
    _Pragma("unroll") for (int u = 0; u < UB; ++u) _Pragma("unroll") for (int j = 0; j < 3; ++j) Vn[u][j] = __builtin_nontemporal_load(reinterpret_cast<const u32x4*>(vq_ + u * 8 * DH + 64 * j)); } while (0)
  float m = -INFINITY, l = 0.f, acc[24];
#pragma unroll
  for (int k = 0; k < 24; ++k) acc[k] = 0.f;
  ISSUE(0);
  for (int it = 0; it < NIT; ++it) {
    u32x4 Kc[UB][3], Vc[UB][3];
#pragma unroll
    for (int u = 0; u < UB; ++u)
#pragma unroll
      for (int j = 0; j < 3; ++j) { Kc[u][j] = Kn[u][j]; Vc[u][j] = Vn[u][j]; }
    if (it + 1 < NIT) ISSUE(it + 1);
    float s[UB];
#pragma unroll
    for (int u = 0; u < UB; ++u) {
      float d = 0.f;
#pragma unroll
      for (int j = 0; j < 3; ++j)
#pragma unroll
        for (int k = 0; k < 4; ++k) d = dot2(Kc[u][j][k], qf[j][k], d);
      d += dpp<0xB1>(d);
      d += dpp<0x4E>(d);
      d += dpp<0x141>(d);
      s[u] = rbf(rbf(d) * SCALE) * LOG2E;
    }
    float mn = m;
#pragma unroll
    for (int u = 0; u < UB; ++u) mn = fmaxf(mn, s[u]);
    const float alpha = __builtin_amdgcn_exp2f(m - mn);
    m = mn;
    float p[UB];
    float ls = 0.f;
#pragma unroll
    for (int u = 0; u < UB; ++u) { p[u] = __builtin_amdgcn_exp2f(s[u] - mn); ls += p[u]; }
    l = fmaf(l, alpha, ls);
#pragma unroll
    for (int k = 0; k < 24; ++k) acc[k] *= alpha;
#pragma unroll
    for (int u = 0; u < UB; ++u)
#pragma unroll
      for (int j = 0; j < 3; ++j)
#pragma unroll
        for (int k = 0; k < 4; ++k) {
          acc[j * 8 + 2 * k] = fmaf(p[u], lo_f(Vc[u][j][k]), acc[j * 8 + 2 * k]);
          acc[j * 8 + 2 * k + 1] = fmaf(p[u], hi_f(Vc[u][j][k]), acc[j * 8 + 2 * k + 1]);
        }
  }
#undef ISSUE
  float M = m;
  for (int o = 8; o < 64; o <<= 1) M = fmaxf(M, __shfl_xor(M, o));
  const float sc = __builtin_amdgcn_exp2f(m - M);
  l *= sc;
  for (int o = 8; o < 64; o <<= 1) l += __shfl_xor(l, o);
#pragma unroll
  for (int k = 0; k < 24; ++k) {
    float a = acc[k] * sc;
    for (int o = 8; o < 64; o <<= 1) a += __shfl_xor(a, o);
    acc[k] = a;
  }
  if (r == 0) {
#pragma unroll
    for (int j = 0; j < 3; ++j) {
      *reinterpret_cast<f32x4*>(&mrg[wave][8 + 64 * j + 8 * c]) = f32x4{acc[j * 8], acc[j * 8 + 1], acc[j * 8 + 2], acc[j * 8 + 3]};
      *reinterpret_cast<f32x4*>(&mrg[wave][8 + 64 * j + 8 * c + 4]) = f32x4{acc[j * 8 + 4], acc[j * 8 + 5], acc[j * 8 + 6], acc[j * 8 + 7]};
    }
    if (c == 0) { mrg[wave][0] = M; mrg[wave][1] = l; }
  }
  __syncthreads();
  if (t < DH) {
    float MM = -INFINITY;
#pragma unroll
    for (int w = 0; w < WAVES_B; ++w) MM = fmaxf(MM, mrg[w][0]);
    float L = 0.f, o = 0.f;
#pragma unroll
    for (int w = 0; w < WAVES_B; ++w) {
      const float wt = __builtin_amdgcn_exp2f(mrg[w][0] - MM);
      L = fmaf(wt, mrg[w][1], L);
      o = fmaf(wt, mrg[w][8 + t], o);
    }
    float* rec = reinterpret_cast<float*>(ws + WS_PART) + ((size_t)h * NSP + sp) * PREC;
    rec[8 + t] = o;
    if (t == 0) { rec[0] = MM; rec[1] = L; }
  }
}

__global__ void __launch_bounds__(256) kC(const __hip_bfloat16* __restrict__ x, const __hip_bfloat16* __restrict__ wo,
                                          const __hip_bfloat16* __restrict__ w2, unsigned char* __restrict__ ws,
                                          __hip_bfloat16* __restrict__ out) {
  __shared__ __attribute__((aligned(16))) float vec[DH];
  __shared__ __attribute__((aligned(16))) float part[4][128];
  __shared__ unsigned lastflag;
  const int ct = blockIdx.x % 5, rt = blockIdx.x / 5;
  const int t = threadIdx.x, lane = t & 63, w = t >> 6;
  const int rs = t >> 4, c = t & 15;
  const __hip_bfloat16* W = (rt < NH) ? wo + (size_t)rt * DH * D_MODEL : w2 + (size_t)(rt - NH) * DH * D_MODEL;
  u32x4 a[12];
  {
    const __hip_bfloat16* p = W + (size_t)rs * D_MODEL + ct * 128 + c * 8;
#pragma unroll
    for (int i = 0; i < 12; ++i) a[i] = *reinterpret_cast<const u32x4*>(p + (size_t)i * 16 * D_MODEL);
  }
  if (t < DH) {
    if (rt < NH) {
      const float* rec = reinterpret_cast<const float*>(ws + WS_PART) + (size_t)rt * NSP * PREC;
      float MM = -INFINITY;
      for (int s = 0; s < NSP; ++s) MM = fmaxf(MM, rec[s * PREC]);
      float L = 0.f, o = 0.f;
      for (int s = 0; s < NSP; ++s) {
        const float wt = __builtin_amdgcn_exp2f(rec[s * PREC] - MM);
        L = fmaf(wt, rec[s * PREC + 1], L);
        o = fmaf(wt, rec[s * PREC + 8 + t], o);
      }
      vec[t] = rbf(o / L);
    } else {
      const float* gp = reinterpret_cast<const float*>(ws + WS_GP) + (rt - NH) * DH + t;
      float g = 0.f, u = 0.f;
#pragma unroll
      for (int sl = 0; sl < 8; ++sl) { g += gp[sl * FF]; u += gp[(8 + sl) * FF]; }
      vec[t] = rbf(rbf(silu_f(rbf(g))) * rbf(u));
    }
  }
  __syncthreads();
  float acc[8] = {0, 0, 0, 0, 0, 0, 0, 0};
#pragma unroll
  for (int i = 0; i < 12; ++i) {
    const float xv = vec[rs + 16 * i];
#pragma unroll
    for (int k = 0; k < 4; ++k) { acc[2 * k] = fmaf(xv, lo_f(a[i][k]), acc[2 * k]); acc[2 * k + 1] = fmaf(xv, hi_f(a[i][k]), acc[2 * k + 1]); }
  }
#pragma unroll
  for (int k = 0; k < 8; ++k) { acc[k] += __shfl_xor(acc[k], 16); acc[k] += __shfl_xor(acc[k], 32); }
  if (lane < 16) {
#pragma unroll
    for (int k = 0; k < 8; ++k) part[w][c * 8 + k] = acc[k];
  }
  __syncthreads();
  float* cp = reinterpret_cast<float*>(ws + WS_CP) + (size_t)ct * 40 * 128;
  if (t < 128) {
    const float s = part[0][t] + part[1][t] + part[2][t] + part[3][t];
    __hip_atomic_store(reinterpret_cast<unsigned*>(cp + rt * 128 + t), __float_as_uint(s), __ATOMIC_RELAXED, __HIP_MEMORY_SCOPE_AGENT);
  }
  asm volatile("s_waitcnt vmcnt(0)" ::: "memory");
  __syncthreads();
  if (t == 0) {
    unsigned* cnt = reinterpret_cast<unsigned*>(ws + WS_CNT) + ct * 64;
    const unsigned old = __hip_atomic_fetch_add(cnt, 1u, __ATOMIC_RELAXED, __HIP_MEMORY_SCOPE_AGENT);
    const unsigned last = (old == 39u) ? 1u : 0u;
    if (last) { __builtin_amdgcn_fence(__ATOMIC_ACQUIRE, "agent"); asm volatile("s_waitcnt vmcnt(0)" ::: "memory"); }
    lastflag = last;
  }
  __syncthreads();
  if (lastflag == 0u) return;
  if (t < 128) {
    float s = 0.f, f = 0.f;
    for (int i = 0; i < NH; ++i) s += __uint_as_float(__hip_atomic_load(reinterpret_cast<unsigned*>(cp + i * 128 + t), __ATOMIC_RELAXED, __HIP_MEMORY_SCOPE_AGENT));
    for (int i = NH; i < 40; ++i) f += __uint_as_float(__hip_atomic_load(reinterpret_cast<unsigned*>(cp + i * 128 + t), __ATOMIC_RELAXED, __HIP_MEMORY_SCOPE_AGENT));
    const float g = rbf(silu_f(rbf(f)));
    const int col = ct * 128 + t;
    out[col] = __float2bfloat16(__bfloat162float(x[col]) + rbf(rbf(s) * g));
  }
}

}

extern "C" void kernel_launch(void* const* d_in, const int* in_sizes, int n_in, void* d_out, int out_size, void* d_ws,
                              size_t ws_size, hipStream_t stream) {
  const __hip_bfloat16* x = (const __hip_bfloat16*)d_in[0];
  const __hip_bfloat16* kc = (const __hip_bfloat16*)d_in[1];
  const __hip_bfloat16* vc = (const __hip_bfloat16*)d_in[2];
  const __hip_bfloat16* wq = (const __hip_bfloat16*)d_in[3];
  const __hip_bfloat16* wo = (const __hip_bfloat16*)d_in[4];
  const __hip_bfloat16* w1 = (const __hip_bfloat16*)d_in[6];
  const __hip_bfloat16* w3 = (const __hip_bfloat16*)d_in[7];
  const __hip_bfloat16* w2 = (const __hip_bfloat16*)d_in[8];
  unsigned char* ws = (unsigned char*)d_ws;
  hipLaunchKernelGGL(kA, dim3(384), dim3(256), 0, stream, x, wq, w1, w3, ws);
  hipLaunchKernelGGL(kB, dim3(NH * NSP), dim3(256), 0, stream, kc, vc, ws);
  hipLaunchKernelGGL(kC, dim3(200), dim3(256), 0, stream, x, wo, w2, ws, (__hip_bfloat16*)d_out);
}
```

```cpp
#include <hip/hip_runtime.h>
#include <hip/hip_bf16.h>
#include <cstdint>

namespace {

constexpr int D_MODEL = 640, NH = 32, SK = 32768, DH = 192, FF = 1536, HD = NH * DH;
#define NSPLIT 16
constexpr int NSP = NSPLIT;
constexpr int KEYS_PER_WG = SK / NSP;
constexpr int PREC = 200;
constexpr size_t WS_Q = 0;
constexpr size_t WS_H = 16384;
constexpr size_t WS_CNT = 32768;
constexpr size_t WS_PART = 65536;
constexpr size_t WS_CP = WS_PART + (size_t)NH * 64 * PREC * 4;
constexpr size_t WS_QP = WS_CP + 5 * 40 * 128 * 4;
constexpr size_t WS_GP = WS_QP + (size_t)NH * 64 * DH * 4;

using u32x4 = __attribute__((ext_vector_type(4))) unsigned;
using f32x4 = __attribute__((ext_vector_type(4))) float;
using bf16x2 = __attribute__((ext_vector_type(2))) __bf16;

__device__ __forceinline__ float bf2f(unsigned short b) { return __uint_as_float(((unsigned)b) << 16); }
__device__ __forceinline__ float rbf(float x) {
  __hip_bfloat16 b = __float2bfloat16(x);
  return __bfloat162float(b);
}
__device__ __forceinline__ unsigned short f2bfbits(float x) {
  __hip_bfloat16 b = __float2bfloat16(x);
  return __builtin_bit_cast(unsigned short, b);
}
__device__ __forceinline__ float dot2(unsigned k, unsigned q, float acc) {
  return __builtin_amdgcn_fdot2_f32_bf16(__builtin_bit_cast(bf16x2, k), __builtin_bit_cast(bf16x2, q), acc, false);
}
template <int CTRL> __device__ __forceinline__ float dpp(float x) {
  return __builtin_bit_cast(float, __builtin_amdgcn_mov_dpp(__builtin_bit_cast(int, x), CTRL, 0xf, 0xf, true));
}
__device__ __forceinline__ float lo_f(unsigned v) { return __uint_as_float(v << 16); }
__device__ __forceinline__ float hi_f(unsigned v) { return __uint_as_float(v & 0xffff0000u); }
__device__ __forceinline__ float silu_f(float x) { return x / (1.f + __expf(-x)); }

__device__ __forceinline__ void layernorm_to_lds(const __hip_bfloat16* __restrict__ x, float* xn, float* red) {
  const int t = threadIdx.x, lane = t & 63, w = t >> 6;
  float v0 = __bfloat162float(x[t]), v1 = __bfloat162float(x[t + 256]);
  float v2 = (t < 128) ? __bfloat162float(x[t + 512]) : 0.f;
  float s = v0 + v1 + v2;
  for (int o = 32; o; o >>= 1) s += __shfl_xor(s, o);
  if (lane == 0) red[w] = s;
  __syncthreads();
  const float mean = (red[0] + red[1] + red[2] + red[3]) * (1.f / D_MODEL);
  float d0 = v0 - mean, d1 = v1 - mean, d2 = (t < 128) ? v2 - mean : 0.f;
  float q = d0 * d0 + d1 * d1 + d2 * d2;
  for (int o = 32; o; o >>= 1) q += __shfl_xor(q, o);
  if (lane == 0) red[4 + w] = q;
  __syncthreads();
  const float var = (red[4] + red[5] + red[6] + red[7]) * (1.f / D_MODEL);
  const float mb = rbf(mean), vb = rbf(var);
  const float den = rbf(sqrtf(rbf(vb + 1e-5f)));
  xn[t] = rbf(rbf(v0 - mb) / den);
  xn[t + 256] = rbf(rbf(v1 - mb) / den);
  if (t < 128) xn[t + 512] = rbf(rbf(v2 - mb) / den);
  __syncthreads();
}

__device__ __forceinline__ void gate_up_wg(int b, const __hip_bfloat16* __restrict__ x,
                                          const __hip_bfloat16* __restrict__ w1, const __hip_bfloat16* __restrict__ w3,
                                          unsigned char* __restrict__ ws, float* xn, float* red, float (*part)[PREC]) {
  const int t = threadIdx.x, lane = t & 63, w = t >> 6;
  const int rg = t >> 3, c = t & 7;
  const int sl = b & 7;
  const __hip_bfloat16* W; int ld, col0; float* dst;
  { const int m = (b) >> 6, tile = ((b) >> 3) & 7; W = m ? w3 : w1; ld = FF; col0 = tile * DH;
         dst = reinterpret_cast<float*>(ws + WS_GP) + (m * 8 + sl) * FF + col0; }
  const __hip_bfloat16* p = W + (size_t)(sl * 80 + rg) * ld + col0 + c * 8;
  u32x4 a[3][3];
#pragma unroll
  for (int i = 0; i < 3; ++i)
#pragma unroll
    for (int j = 0; j < 3; ++j)
      a[i][j] = (i < 2 || rg < 16) ? *reinterpret_cast<const u32x4*>(p + (size_t)i * 32 * ld + 64 * j) : u32x4{0u, 0u, 0u, 0u};
  layernorm_to_lds(x, xn, red);
  float acc[24];
#pragma unroll
  for (int k = 0; k < 24; ++k) acc[k] = 0.f;
#pragma unroll
  for (int i = 0; i < 3; ++i) {
    const float xv = (i < 2 || rg < 16) ? xn[sl * 80 + rg + 32 * i] : 0.f;
#pragma unroll
    for (int j = 0; j < 3; ++j)
#pragma unroll
      for (int k = 0; k < 4; ++k) { acc[j * 8 + 2 * k] = fmaf(xv, lo_f(a[i][j][k]), acc[j * 8 + 2 * k]); acc[j * 8 + 2 * k + 1] = fmaf(xv, hi_f(a[i][j][k]), acc[j * 8 + 2 * k + 1]); }
  }
#pragma unroll
  for (int k = 0; k < 24; ++k) for (int o = 8; o < 64; o <<= 1) acc[k] += __shfl_xor(acc[k], o);
  if (lane < 8) {
#pragma unroll
    for (int j = 0; j < 3; ++j)
#pragma unroll
      for (int k = 0; k < 8; ++k) part[w][64 * j + 8 * c + k] = acc[j * 8 + k];
  }
  __syncthreads();
  if (t < DH) dst[t] = part[0][t] + part[1][t] + part[2][t] + part[3][t];
}

constexpr int UB = 2;
constexpr int ROWS_IT = 8 * UB;
constexpr int WAVES_B = 4;
constexpr int KEYS_PER_WAVE = KEYS_PER_WG / WAVES_B;
constexpr int NIT = KEYS_PER_WAVE / ROWS_IT;

__global__ void __launch_bounds__(256, 2) kB(const __hip_bfloat16* __restrict__ kc, const __hip_bfloat16* __restrict__ vc,
                                             const __hip_bfloat16* __restrict__ x, const __hip_bfloat16* __restrict__ wq,
                                             const __hip_bfloat16* __restrict__ w1, const __hip_bfloat16* __restrict__ w3,
                                             unsigned char* __restrict__ ws) {
  __shared__ __attribute__((aligned(16))) float mrg[WAVES_B][PREC];
  __shared__ float xn[D_MODEL];
  __shared__ float red[8];
  __shared__ __attribute__((aligned(16))) unsigned short qs[DH];
  if (blockIdx.x < 128) { gate_up_wg(blockIdx.x, x, w1, w3, ws, xn, red, mrg); return; }
  const int bid = blockIdx.x - 128;
  const int h = bid / NSP, sp = bid % NSP;
  const int t = threadIdx.x, lane = t & 63, wave = __builtin_amdgcn_readfirstlane(t >> 6);
  const int r = lane >> 3, c = lane & 7;
  constexpr float SCALE = 0.07216878364870322f;
  constexpr float LOG2E = 1.4426950408889634f;
  const size_t key0 = (size_t)h * SK + (size_t)sp * KEYS_PER_WG + (size_t)wave * KEYS_PER_WAVE;
  const __hip_bfloat16* kp = kc + (key0 + r) * DH + c * 8;
  const __hip_bfloat16* vp = vc + (key0 + r) * DH + c * 8;
  u32x4 Kn[UB][3], Vn[UB][3];
#define ISSUE(it_) do { const __hip_bfloat16* kq_ = kp + (size_t)(it_) * ROWS_IT * DH; const __hip_bfloat16* vq_ = vp + (size_t)(it_) * ROWS_IT * DH; \
    _Pragma("unroll") for (int u = 0; u < UB; ++u) _Pragma("unroll") for (int j = 0; j < 3; ++j) Kn[u][j] = __builtin_nontemporal_load(reinterpret_cast<const u32x4*>(kq_ + u * 8 * DH + 64 * j)); \
    _Pragma("unroll") for (int u = 0; u < UB; ++u) _Pragma("unroll") for (int j = 0; j < 3; ++j) Vn[u][j] = __builtin_nontemporal_load(reinterpret_cast<const u32x4*>(vq_ + u * 8 * DH + 64 * j)); } while (0)
  float m = -INFINITY, l = 0.f, acc[24];
#pragma unroll
  for (int k = 0; k < 24; ++k) acc[k] = 0.f;
  constexpr int QROWS = D_MODEL / NSP;
  u32x4 Kc[UB][3], Vc[UB][3];
  {
    const int rg = t >> 3;
    const __hip_bfloat16* wp = wq + (size_t)(sp * QROWS + rg) * HD + h * DH + c * 8;
    u32x4 a[2][3];
#pragma unroll
    for (int i = 0; i < 2; ++i)
#pragma unroll
      for (int j = 0; j < 3; ++j)
        a[i][j] = (rg + 32 * i < QROWS) ? *reinterpret_cast<const u32x4*>(wp + (size_t)i * 32 * HD + 64 * j) : u32x4{0u, 0u, 0u, 0u};
    ISSUE(0);
    layernorm_to_lds(x, xn, red);
    float qa[24];
#pragma unroll
    for (int k = 0; k < 24; ++k) qa[k] = 0.f;
#pragma unroll
    for (int i = 0; i < 2; ++i) {
      const float xv = (rg + 32 * i < QROWS) ? xn[sp * QROWS + rg + 32 * i] : 0.f;
#pragma unroll
      for (int j = 0; j < 3; ++j)
#pragma unroll
        for (int k = 0; k < 4; ++k) { qa[j * 8 + 2 * k] = fmaf(xv, lo_f(a[i][j][k]), qa[j * 8 + 2 * k]); qa[j * 8 + 2 * k + 1] = fmaf(xv, hi_f(a[i][j][k]), qa[j * 8 + 2 * k + 1]); }
    }
#pragma unroll
    for (int k = 0; k < 24; ++k) for (int o = 8; o < 64; o <<= 1) qa[k] += __shfl_xor(qa[k], o);
    if (r == 0) {
#pragma unroll
      for (int j = 0; j < 3; ++j)
#pragma unroll
        for (int k = 0; k < 8; ++k) mrg[wave][64 * j + 8 * c + k] = qa[j * 8 + k];
    }
    __syncthreads();
    unsigned* qp = reinterpret_cast<unsigned*>(ws + WS_QP) + (size_t)h * NSP * DH;
    if (t < DH) __hip_atomic_store(qp + sp * DH + t, __float_as_uint(mrg[0][t] + mrg[1][t] + mrg[2][t] + mrg[3][t]), __ATOMIC_RELAXED, __HIP_MEMORY_SCOPE_AGENT);
    asm volatile("s_waitcnt vmcnt(0)" ::: "memory");
#pragma unroll
    for (int u = 0; u < UB; ++u)
#pragma unroll
      for (int j = 0; j < 3; ++j) { Kc[u][j] = Kn[u][j]; Vc[u][j] = Vn[u][j]; }
    ISSUE(1);
    __syncthreads();
    if (t == 0) {
      unsigned* cnt = reinterpret_cast<unsigned*>(ws + WS_CNT) + 1024 + h * 64;
      __hip_atomic_fetch_add(cnt, 1u, __ATOMIC_RELAXED, __HIP_MEMORY_SCOPE_AGENT);
      while (__hip_atomic_load(cnt, __ATOMIC_RELAXED, __HIP_MEMORY_SCOPE_AGENT) < (unsigned)NSP) __builtin_amdgcn_s_sleep(2);
    }
    __syncthreads();
    if (t < DH) {
      float sq = 0.f;
#pragma unroll
      for (int i = 0; i < NSP; ++i) sq += __uint_as_float(__hip_atomic_load(qp + i * DH + t, __ATOMIC_RELAXED, __HIP_MEMORY_SCOPE_AGENT));
      qs[t] = f2bfbits(sq);
    }
    __syncthreads();
  }
  u32x4 qf[3];
#pragma unroll
  for (int j = 0; j < 3; ++j) qf[j] = *reinterpret_cast<const u32x4*>(qs + c * 8 + 64 * j);
  for (int it = 0; it < NIT; ++it) {
    float s[UB];
#pragma unroll
    for (int u = 0; u < UB; ++u) {
      float d = 0.f;
#pragma unroll
      for (int j = 0; j < 3; ++j)
#pragma unroll
        for (int k = 0; k < 4; ++k) d = dot2(Kc[u][j][k], qf[j][k], d);
      d += dpp<0xB1>(d);
      d += dpp<0x4E>(d);
      d += dpp<0x141>(d);
      s[u] = rbf(rbf(d) * SCALE) * LOG2E;
    }
    float mn = m;
#pragma unroll
    for (int u = 0; u < UB; ++u) mn = fmaxf(mn, s[u]);
    const float alpha = __builtin_amdgcn_exp2f(m - mn);
    m = mn;
    float p[UB];
    float ls = 0.f;
#pragma unroll
    for (int u = 0; u < UB; ++u) { p[u] = __builtin_amdgcn_exp2f(s[u] - mn); ls += p[u]; }
    l = fmaf(l, alpha, ls);
#pragma unroll
    for (int k = 0; k < 24; ++k) acc[k] *= alpha;
#pragma unroll
    for (int u = 0; u < UB; ++u)
#pragma unroll
      for (int j = 0; j < 3; ++j)
#pragma unroll
        for (int k = 0; k < 4; ++k) {
          acc[j * 8 + 2 * k] = fmaf(p[u], lo_f(Vc[u][j][k]), acc[j * 8 + 2 * k]);
          acc[j * 8 + 2 * k + 1] = fmaf(p[u], hi_f(Vc[u][j][k]), acc[j * 8 + 2 * k + 1]);
        }
#pragma unroll
    for (int u = 0; u < UB; ++u)
#pragma unroll
      for (int j = 0; j < 3; ++j) { Kc[u][j] = Kn[u][j]; Vc[u][j] = Vn[u][j]; }
    if (it + 2 < NIT) ISSUE(it + 2);
  }
#undef ISSUE
  float M = m;
  for (int o = 8; o < 64; o <<= 1) M = fmaxf(M, __shfl_xor(M, o));
  const float sc = __builtin_amdgcn_exp2f(m - M);
  l *= sc;
  for (int o = 8; o < 64; o <<= 1) l += __shfl_xor(l, o);
#pragma unroll
  for (int k = 0; k < 24; ++k) {
    float a = acc[k] * sc;
    for (int o = 8; o < 64; o <<= 1) a += __shfl_xor(a, o);
    acc[k] = a;
  }
  if (r == 0) {
#pragma unroll
    for (int j = 0; j < 3; ++j) {
      *reinterpret_cast<f32x4*>(&mrg[wave][8 + 64 * j + 8 * c]) = f32x4{acc[j * 8], acc[j * 8 + 1], acc[j * 8 + 2], acc[j * 8 + 3]};
      *reinterpret_cast<f32x4*>(&mrg[wave][8 + 64 * j + 8 * c + 4]) = f32x4{acc[j * 8 + 4], acc[j * 8 + 5], acc[j * 8 + 6], acc[j * 8 + 7]};
    }
    if (c == 0) { mrg[wave][0] = M; mrg[wave][1] = l; }
  }
  __syncthreads();
  if (t < DH) {
    float MM = -INFINITY;
#pragma unroll
    for (int w = 0; w < WAVES_B; ++w) MM = fmaxf(MM, mrg[w][0]);
    float L = 0.f, o = 0.f;
#pragma unroll
    for (int w = 0; w < WAVES_B; ++w) {
      const float wt = __builtin_amdgcn_exp2f(mrg[w][0] - MM);
      L = fmaf(wt, mrg[w][1], L);
      o = fmaf(wt, mrg[w][8 + t], o);
    }
    float* rec = reinterpret_cast<float*>(ws + WS_PART) + ((size_t)h * NSP + sp) * PREC;
    rec[8 + t] = o;
    if (t == 0) { rec[0] = MM; rec[1] = L; }
  }
}

__global__ void __launch_bounds__(256) kC(const __hip_bfloat16* __restrict__ x, const __hip_bfloat16* __restrict__ wo,
                                          const __hip_bfloat16* __restrict__ w2, unsigned char* __restrict__ ws,
                                          __hip_bfloat16* __restrict__ out) {
  __shared__ __attribute__((aligned(16))) float vec[DH];
  __shared__ __attribute__((aligned(16))) float part[4][128];
  __shared__ unsigned lastflag;
  const int ct = blockIdx.x % 5, rt = blockIdx.x / 5;
  const int t = threadIdx.x, lane = t & 63, w = t >> 6;
  const int rs = t >> 4, c = t & 15;
  const __hip_bfloat16* W = (rt < NH) ? wo + (size_t)rt * DH * D_MODEL : w2 + (size_t)(rt - NH) * DH * D_MODEL;
  u32x4 a[12];
  {
    const __hip_bfloat16* p = W + (size_t)rs * D_MODEL + ct * 128 + c * 8;
#pragma unroll
    for (int i = 0; i < 12; ++i) a[i] = *reinterpret_cast<const u32x4*>(p + (size_t)i * 16 * D_MODEL);
  }
  if (t < DH) {
    if (rt < NH) {
      const float* rec = reinterpret_cast<const float*>(ws + WS_PART) + (size_t)rt * NSP * PREC;
      float MM = -INFINITY;
      for (int s = 0; s < NSP; ++s) MM = fmaxf(MM, rec[s * PREC]);
      float L = 0.f, o = 0.f;
      for (int s = 0; s < NSP; ++s) {
        const float wt = __builtin_amdgcn_exp2f(rec[s * PREC] - MM);
        L = fmaf(wt, rec[s * PREC + 1], L);
        o = fmaf(wt, rec[s * PREC + 8 + t], o);
      }
      vec[t] = rbf(o / L);
    } else {
      const float* gp = reinterpret_cast<const float*>(ws + WS_GP) + (rt - NH) * DH + t;
      float g = 0.f, u = 0.f;
#pragma unroll
      for (int sl = 0; sl < 8; ++sl) { g += gp[sl * FF]; u += gp[(8 + sl) * FF]; }
      vec[t] = rbf(rbf(silu_f(rbf(g))) * rbf(u));
    }
  }
  __syncthreads();
  float acc[8] = {0, 0, 0, 0, 0, 0, 0, 0};
#pragma unroll
  for (int i = 0; i < 12; ++i) {
    const float xv = vec[rs + 16 * i];
#pragma unroll
    for (int k = 0; k < 4; ++k) { acc[2 * k] = fmaf(xv, lo_f(a[i][k]), acc[2 * k]); acc[2 * k + 1] = fmaf(xv, hi_f(a[i][k]), acc[2 * k + 1]); }
  }
#pragma unroll
  for (int k = 0; k < 8; ++k) { acc[k] += __shfl_xor(acc[k], 16); acc[k] += __shfl_xor(acc[k], 32); }
  if (lane < 16) {
#pragma unroll
    for (int k = 0; k < 8; ++k) part[w][c * 8 + k] = acc[k];
  }
  __syncthreads();
  float* cp = reinterpret_cast<float*>(ws + WS_CP) + (size_t)ct * 40 * 128;
  if (t < 128) {
    const float s = part[0][t] + part[1][t] + part[2][t] + part[3][t];
    __hip_atomic_store(reinterpret_cast<unsigned*>(cp + rt * 128 + t), __float_as_uint(s), __ATOMIC_RELAXED, __HIP_MEMORY_SCOPE_AGENT);
  }
  asm volatile("s_waitcnt vmcnt(0)" ::: "memory");
  __syncthreads();
  if (t == 0) {
    unsigned* cnt = reinterpret_cast<unsigned*>(ws + WS_CNT) + ct * 64;
    const unsigned old = __hip_atomic_fetch_add(cnt, 1u, __ATOMIC_RELAXED, __HIP_MEMORY_SCOPE_AGENT);
    const unsigned last = (old == 39u) ? 1u : 0u;
    if (last) { __builtin_amdgcn_fence(__ATOMIC_ACQUIRE, "agent"); asm volatile("s_waitcnt vmcnt(0)" ::: "memory"); }
    lastflag = last;
  }
  __syncthreads();
  if (lastflag == 0u) return;
  if (t < 128) {
    float s = 0.f, f = 0.f;
    for (int i = 0; i < NH; ++i) s += __uint_as_float(__hip_atomic_load(reinterpret_cast<unsigned*>(cp + i * 128 + t), __ATOMIC_RELAXED, __HIP_MEMORY_SCOPE_AGENT));
    for (int i = NH; i < 40; ++i) f += __uint_as_float(__hip_atomic_load(reinterpret_cast<unsigned*>(cp + i * 128 + t), __ATOMIC_RELAXED, __HIP_MEMORY_SCOPE_AGENT));
    const float g = rbf(silu_f(rbf(f)));
    const int col = ct * 128 + t;
    out[col] = __float2bfloat16(__bfloat162float(x[col]) + rbf(rbf(s) * g));
  }
}

}

extern "C" void kernel_launch(void* const* d_in, const int* in_sizes, int n_in, void* d_out, int out_size, void* d_ws,
                              size_t ws_size, hipStream_t stream) {
  const __hip_bfloat16* x = (const __hip_bfloat16*)d_in[0];
  const __hip_bfloat16* kc = (const __hip_bfloat16*)d_in[1];
  const __hip_bfloat16* vc = (const __hip_bfloat16*)d_in[2];
  const __hip_bfloat16* wq = (const __hip_bfloat16*)d_in[3];
  const __hip_bfloat16* wo = (const __hip_bfloat16*)d_in[4];
  const __hip_bfloat16* w1 = (const __hip_bfloat16*)d_in[6];
  const __hip_bfloat16* w3 = (const __hip_bfloat16*)d_in[7];
  const __hip_bfloat16* w2 = (const __hip_bfloat16*)d_in[8];
  unsigned char* ws = (unsigned char*)d_ws;
  hipMemsetAsync(ws + WS_CNT, 0, 16384, stream);
  hipLaunchKernelGGL(kB, dim3(128 + NH * NSP), dim3(256), 0, stream, kc, vc, x, wq, w1, w3, ws);
  hipLaunchKernelGGL(kC, dim3(200), dim3(256), 0, stream, x, wo, w2, ws, (__hip_bfloat16*)d_out);
}
```
